```python
import torch

def _softmax(x, axis=-1):
    return torch.softmax(x, dim=axis)
def _gelu(x):
    return torch.nn.functional.gelu(x, approximate="tanh")
def _silu(x):
    return torch.nn.functional.silu(x)
def _sigmoid(x):
    return torch.sigmoid(x)
def _layernorm(x, axis=-1, eps=1e-5):
    m = x.mean(dim=axis, keepdim=True)
    v = x.var(dim=axis, keepdim=True, unbiased=False)
    return (x - m) / torch.sqrt(v + eps)
def _scatter_add(tgt, idx, src, axis=0):
    return tgt.clone().index_add_(axis, idx, src)
def _segment_sum(src, seg, n):
    out = torch.zeros((n,) + src.shape[1:], dtype=src.dtype)
    return out.index_add_(0, seg, src)
def _pad(x, pw):
    flat = tuple(v for pair in reversed(pw) for v in pair)
    return torch.nn.functional.pad(x, flat)

kernel_name = 'standalone__deepnorm__sampling'

def setup_inputs():

    _w = (lambda a: (lambda t: t.to(torch.bfloat16) if t.is_floating_point() else t)(torch.as_tensor(a)))
    B, S, D = 1024, 32, 512
    return {
        "x": _w((lambda *s: torch.randn(*s, dtype=torch.bfloat16) * 0.05)(B, S, D)),
        "f": _w((lambda *s: torch.randn(*s, dtype=torch.bfloat16) * 0.05)(B, S, D)),
        "g": _w((lambda *s: torch.randn(*s, dtype=torch.bfloat16) * 0.05)(D) + 1.0),
        "b": _w((lambda *s: torch.randn(*s, dtype=torch.bfloat16) * 0.4)(D)),
        "alpha": _w(float(2.0)),
    }

def reference(x, f, g, b, alpha):
    return _layernorm(alpha * x + f, axis=-1) * g + b
```

```cpp
#include <hip/hip_runtime.h>
#include <hip/hip_bf16.h>
#include <stdint.h>

__global__ __launch_bounds__(1024) void deepnorm_kernel(const void* x, const void* f, void* out, void* ws,
                                                        const void* g, const void* b, const void* alpha) {
    __shared__ uint4 lds[132];
    asm volatile(
        "s_load_dwordx8 s[8:15], %0, 0x0\n\t"
        "v_readfirstlane_b32 s24, %2\n\t"
        "v_and_b32 v3, 63, %2\n\t"
        "s_lshr_b32 s30, %1, 3\n\t"
        "s_bfe_u32 s25, %1, 0x10002\n\t"
        "s_lshr_b32 s24, s24, 6\n\t"
        "s_cmp_lg_u32 s24, 1\n\t"
        "s_cbranch_scc1 L_e1%=\n\t"
        "s_getpc_b64 s[90:91]\n\t"
        "v_lshlrev_b32 v9, 7, v3\n\t"
        "s_mov_b64 exec, 0x3fffff\n\t"
        "global_load_dword v10, v9, s[90:91] offset:128\n\t"
        "s_mov_b64 exec, -1\n\t"
        "L_e1%=:\n\t"
        "s_xor_b32 s85, s30, s25\n\t"
        "s_and_b32 s85, s85, 1\n\t"
        "s_lshl_b32 s30, s30, 2\n\t"
        "s_and_b32 s31, %1, 3\n\t"
        "s_or_b32 s30, s30, s31\n\t"
        "s_lshl_b32 s87, s30, 14\n\t"
        "v_lshlrev_b32 v3, 4, v3\n\t"
        "v_or_b32 v2, s87, v3\n\t"
        "s_cmp_lt_u32 s24, 2\n\t"
        "s_cbranch_scc1 L_ctrl%=\n\t"
        "s_xor_b32 s31, s24, s85\n\t"
        "s_and_b32 s31, s31, 1\n\t"
        "s_lshl_b32 s31, s31, 13\n\t"
        "s_mul_i32 s27, s24, 0x40400\n\t"
        "s_sub_u32 s70, s27, 0x80800\n\t"
        "s_and_b32 s70, s70, 0x1e03c00\n\t"
        "s_xor_b32 s70, s70, s31\n\t"
        "v_or_b32 v7, s70, v2\n\t"
        "s_waitcnt lgkmcnt(0)\n\t"
        "global_load_dwordx4 v[62:65], v7, s[8:9] nt\n\t"
        "global_load_dwordx4 v[66:69], v7, s[10:11] nt\n\t"
        "s_branch L_s2%=\n\t"
        "L_ctrl%=:\n\t"
        "s_memrealtime s[22:23]\n\t"
        "s_setprio 3\n\t"
        "v_mov_b32 v4, 0x800\n\t"
        "s_mov_b32 s75, 0\n\t"
        "s_mov_b32 s76, 0\n\t"
        "s_mov_b32 s84, 0\n\t"
        "s_mov_b32 s93, 1\n\t"
        "s_cmp_eq_u32 s24, 1\n\t"
        "s_cbranch_scc1 L_m%=\n\t"
        "s_lshr_b32 s87, %1, 3\n\t"
        "s_mul_i32 s88, s87, 43\n\t"
        "s_lshr_b32 s88, s88, 7\n\t"
        "s_mul_i32 s89, s88, 3\n\t"
        "s_sub_u32 s89, s87, s89\n\t"
        "s_and_b32 s94, s88, 1\n\t"
        "s_mul_i32 s31, %1, 0x9e3779b1\n\t"
        "s_lshl_b32 s28, s94, 13\n\t"
        "s_or_b32 s28, s28, 0x800000\n\t"
        "s_waitcnt lgkmcnt(0)\n\t"
        "s_xor_b32 s31, s31, s22\n\t"
        "s_mul_i32 s31, s31, 0x85ebca6b\n\t"
        "s_lshr_b32 s31, s31, 11\n\t"
        "s_and_b32 s31, s31, 0x1fdf80\n\t"
        "s_or_b32 s31, s31, s28\n\t"
        "s_cmp_eq_u32 s89, 1\n\t"
        "s_cselect_b64 s[90:91], s[10:11], s[8:9]\n\t"
        "s_cmp_eq_u32 s89, 2\n\t"
        "s_cselect_b64 s[90:91], s[12:13], s[90:91]\n\t"
        "s_xor_b32 s28, s31, 0x2000\n\t"
        "s_load_dword s80, s[90:91], s31\n\t"
        "s_load_dwordx2 s[34:35], s[90:91], s28\n\t"
        "s_waitcnt lgkmcnt(2)\n\t"
        "s_nop 15\n\t"
        "s_getreg_b32 s83, hwreg(HW_REG_IB_STS, 8, 4)\n\t"
        "s_branch L_p%=\n\t"
        "L_s2%=:\n\t"
        "s_mov_b64 exec, 0\n\t"
        "s_cmp_eq_u32 s24, 2\n\t"
        "s_cbranch_scc1 L_t1%=\n\t"
        "s_cmp_eq_u32 s24, 3\n\t"
        "s_cbranch_scc1 L_t2%=\n\t"
        "L_tb%=:\n\t"
        "s_mov_b64 exec, -1\n\t"
        "s_add_u32 s71, s27, 0x303000\n\t"
        "s_and_b32 s71, s71, 0x1e03c00\n\t"
        "s_xor_b32 s71, s71, s31\n\t"
        "s_xor_b32 s71, s71, 0x2000\n\t"
        "v_or_b32 v8, s71, v2\n\t"
        "s_add_u32 s28, s27, 0x686800\n\t"
        "s_and_b32 s28, s28, 0x1e03c00\n\t"
        "s_xor_b32 s28, s28, s31\n\t"
        "v_or_b32 v9, s28, v2\n\t"
        "s_bitcmp1_b32 s24, 0\n\t"
        "s_mov_b32 s76, 5\n\t"
        "s_cselect_b32 s76, 10, s76\n\t"
        "s_mov_b32 s75, 2\n\t"
        "s_mov_b32 s84, 1\n\t"
        "s_waitcnt vmcnt(0)\n\t"
        "global_load_dwordx4 v[70:73], v8, s[8:9] nt\n\t"
        "global_load_dwordx4 v[74:77], v8, s[10:11] nt\n\t"
        "s_waitcnt vmcnt(0)\n\t"
        "global_load_dwordx4 v[12:15], v9, s[8:9] nt\n\t"
        "global_load_dwordx4 v[16:19], v9, s[10:11] nt\n\t"
        "s_barrier\n\t"
        "L_common%=:\n\t"
        "v_mov_b32 v4, 0x800\n\t"
        "ds_read_b32 v9, v4 offset:8\n\t"
        "ds_read_b32 v10, v4 offset:16\n\t"
        "ds_read_b128 v[36:39], v3\n\t"
        "ds_read_b128 v[40:43], v3 offset:1024\n\t"
        "v_mov_b32 v5, 1\n\t"
        "v_mov_b32 v60, 0xbb000000\n\t"
        "v_mov_b32 v61, 0x3727c5ac\n\t"
        "s_mov_b32 s81, 0xffff0000\n\t"
        "s_mov_b32 s74, 0\n\t"
        "s_waitcnt lgkmcnt(0)\n\t"
        "v_readfirstlane_b32 s26, v9\n\t"
        "v_readfirstlane_b32 s80, v10\n\t"
        "v_lshlrev_b32 v44, 16, v36\n\t"
        "v_and_b32 v45, s81, v36\n\t"
        "v_lshlrev_b32 v46, 16, v37\n\t"
        "v_and_b32 v47, s81, v37\n\t"
        "v_lshlrev_b32 v48, 16, v38\n\t"
        "v_and_b32 v49, s81, v38\n\t"
        "v_lshlrev_b32 v50, 16, v39\n\t"
        "v_and_b32 v51, s81, v39\n\t"
        "v_lshlrev_b32 v52, 16, v40\n\t"
        "v_and_b32 v53, s81, v40\n\t"
        "v_lshlrev_b32 v54, 16, v41\n\t"
        "v_and_b32 v55, s81, v41\n\t"
        "v_lshlrev_b32 v56, 16, v42\n\t"
        "v_and_b32 v57, s81, v42\n\t"
        "v_lshlrev_b32 v58, 16, v43\n\t"
        "v_and_b32 v59, s81, v43\n\t"
        "s_lshl_b32 s80, s80, 16\n\t"
        "s_waitcnt vmcnt(0)\n\t"
        "s_branch L_body%=\n\t"
        "L_m%=:\n\t"
        "s_load_dwordx4 s[16:19], %0, 0x20\n\t"
        "s_load_dwordx2 s[20:21], %0, 0x30\n\t"
        "v_mov_b32 v22, 42\n\t"
        "v_mov_b32 v23, 42\n\t"
        "v_mov_b32 v24, -1\n\t"
        "ds_write_b64 v4, v[22:23]\n\t"
        "ds_write_b32 v4, v24 offset:12\n\t"
        "v_mov_b32 v5, 0\n\t"
        "s_lshl_b32 s30, s30, 8\n\t"
        "s_add_u32 s30, s30, 0x10000\n\t"
        "s_waitcnt lgkmcnt(0)\n\t"
        "s_add_u32 s88, s14, s30\n\t"
        "s_addc_u32 s89, s15, 0\n\t"
        "s_cmp_ge_u32 %1, 16\n\t"
        "s_cbranch_scc1 L_spt%=\n\t"
        "s_load_dword s36, s[14:15], 0x18000\n\t"
        "s_load_dword s37, s[16:17], 0x0\n\t"
        "s_load_dword s38, s[18:19], 0x0\n\t"
        "L_spt%=:\n\t"
        "s_sleep 16\n\t"
        "global_load_dwordx4 v[12:15], v3, s[16:17]\n\t"
        "global_load_dwordx4 v[16:19], v3, s[18:19]\n\t"
        "global_load_ushort v20, v5, s[20:21]\n\t"
        "s_mov_b64 exec, 1\n\t"
        "global_load_dword v21, v5, s[88:89] sc1\n\t"
        "s_mov_b64 exec, -1\n\t"
        "s_waitcnt vmcnt(1)\n\t"
        "ds_write_b128 v3, v[12:15]\n\t"
        "ds_write_b128 v3, v[16:19] offset:1024\n\t"
        "ds_write_b32 v4, v20 offset:16\n\t"
        "s_waitcnt lgkmcnt(0)\n\t"
        "s_barrier\n\t"
        "s_branch L_m2%=\n\t"
        "L_loop%=:\n\t"
        "s_waitcnt vmcnt(1)\n\t"
        "L_body%=:\n\t"
        "s_mov_b64 exec, 1\n\t"
        "ds_add_rtn_u32 v6, v4, v5\n\t"
        "s_mov_b64 exec, -1\n\t"
        "v_lshlrev_b32 v28, 16, v12\n\t"
        "v_and_b32 v29, s81, v12\n\t"
        "v_lshlrev_b32 v20, 16, v16\n\t"
        "v_and_b32 v21, s81, v16\n\t"
        "v_lshlrev_b32 v30, 16, v13\n\t"
        "v_and_b32 v31, s81, v13\n\t"
        "v_lshlrev_b32 v22, 16, v17\n\t"
        "v_and_b32 v23, s81, v17\n\t"
        "v_lshlrev_b32 v32, 16, v14\n\t"
        "v_and_b32 v33, s81, v14\n\t"
        "v_lshlrev_b32 v24, 16, v18\n\t"
        "v_and_b32 v25, s81, v18\n\t"
        "v_lshlrev_b32 v34, 16, v15\n\t"
        "v_and_b32 v35, s81, v15\n\t"
        "v_lshlrev_b32 v26, 16, v19\n\t"
        "v_and_b32 v27, s81, v19\n\t"
        "s_waitcnt lgkmcnt(0)\n\t"
        "v_readfirstlane_b32 s27, v6\n\t"
        "s_cmp_ge_u32 s27, 128\n\t"
        "s_cbranch_scc1 L_noload%=\n\t"
        "s_mul_i32 s29, s27, 0x40400\n\t"
        "s_and_b32 s29, s29, 0x1e03c00\n\t"
        "s_xor_b32 s29, s29, s26\n\t"
        "v_or_b32 v7, s29, v2\n\t"
        "global_load_dwordx4 v[12:15], v7, s[8:9] nt\n\t"
        "global_load_dwordx4 v[16:19], v7, s[10:11] nt\n\t"
        "L_noload%=:\n\t"
        "s_cmp_lg_u32 s84, 1\n\t"
        "s_cbranch_scc1 L_join%=\n\t"
        "L_go%=:\n\t"
        "v_pk_fma_f32 v[20:21], s[80:81], v[28:29], v[20:21] op_sel_hi:[0,1,1]\n\t"
        "v_pk_fma_f32 v[22:23], s[80:81], v[30:31], v[22:23] op_sel_hi:[0,1,1]\n\t"
        "v_pk_fma_f32 v[24:25], s[80:81], v[32:33], v[24:25] op_sel_hi:[0,1,1]\n\t"
        "v_pk_fma_f32 v[26:27], s[80:81], v[34:35], v[26:27] op_sel_hi:[0,1,1]\n\t"
        "v_pk_add_f32 v[40:41], v[20:21], v[22:23]\n\t"
        "v_or_b32 v8, s28, v2\n\t"
        "v_pk_add_f32 v[42:43], v[24:25], v[26:27]\n\t"
        "s_mov_b32 s28, s29\n\t"
        "v_pk_add_f32 v[40:41], v[40:41], v[42:43]\n\t"
        "s_nop 0\n\t"
        "v_add_f32 v9, v40, v41\n\t"
        "s_nop 1\n\t"
        "v_add_f32_dpp v9, v9, v9 quad_perm:[1,0,3,2] row_mask:0xf bank_mask:0xf\n\t"
        "s_nop 1\n\t"
        "v_add_f32_dpp v9, v9, v9 quad_perm:[2,3,0,1] row_mask:0xf bank_mask:0xf\n\t"
        "L_t1%=:\n\t"
        "s_cbranch_execz L_tb%=\n\t"
        "s_nop 0\n\t"
        "v_add_f32_dpp v9, v9, v9 row_half_mirror row_mask:0xf bank_mask:0xf\n\t"
        "s_nop 1\n\t"
        "v_add_f32_dpp v9, v9, v9 row_mirror row_mask:0xf bank_mask:0xf\n\t"
        "s_nop 1\n\t"
        "v_add_f32_dpp v9, v9, v9 row_bcast:15 row_mask:0xa bank_mask:0xf\n\t"
        "s_nop 1\n\t"
        "v_add_f32_dpp v9, v9, v9 row_bcast:31 row_mask:0xc bank_mask:0xf\n\t"
        "s_nop 0\n\t"
        "v_readlane_b32 s82, v9, 63\n\t"
        "s_nop 1\n\t"
        "v_pk_fma_f32 v[20:21], s[82:83], v[60:61], v[20:21] op_sel_hi:[0,0,1]\n\t"
        "v_pk_fma_f32 v[22:23], s[82:83], v[60:61], v[22:23] op_sel_hi:[0,0,1]\n\t"
        "v_pk_fma_f32 v[24:25], s[82:83], v[60:61], v[24:25] op_sel_hi:[0,0,1]\n\t"
        "v_pk_fma_f32 v[26:27], s[82:83], v[60:61], v[26:27] op_sel_hi:[0,0,1]\n\t"
        "v_pk_mul_f32 v[40:41], v[20:21], v[20:21]\n\t"
        "v_pk_mul_f32 v[42:43], v[22:23], v[22:23]\n\t"
        "v_pk_fma_f32 v[40:41], v[24:25], v[24:25], v[40:41]\n\t"
        "v_pk_fma_f32 v[42:43], v[26:27], v[26:27], v[42:43]\n\t"
        "s_nop 0\n\t"
        "v_pk_add_f32 v[40:41], v[40:41], v[42:43]\n\t"
        "s_nop 0\n\t"
        "v_add_f32 v9, v40, v41\n\t"
        "v_pk_mul_f32 v[20:21], v[20:21], v[44:45]\n\t"
        "s_nop 0\n\t"
        "v_add_f32_dpp v9, v9, v9 quad_perm:[1,0,3,2] row_mask:0xf bank_mask:0xf\n\t"
        "v_pk_mul_f32 v[22:23], v[22:23], v[46:47]\n\t"
        "s_nop 0\n\t"
        "v_add_f32_dpp v9, v9, v9 quad_perm:[2,3,0,1] row_mask:0xf bank_mask:0xf\n\t"
        "v_pk_mul_f32 v[24:25], v[24:25], v[48:49]\n\t"
        "s_nop 0\n\t"
        "v_add_f32_dpp v9, v9, v9 row_half_mirror row_mask:0xf bank_mask:0xf\n\t"
        "v_pk_mul_f32 v[26:27], v[26:27], v[50:51]\n\t"
        "s_nop 0\n\t"
        "v_add_f32_dpp v9, v9, v9 row_mirror row_mask:0xf bank_mask:0xf\n\t"
        "s_nop 1\n\t"
        "v_add_f32_dpp v9, v9, v9 row_bcast:15 row_mask:0xa bank_mask:0xf\n\t"
        "s_nop 1\n\t"
        "v_add_f32_dpp v9, v9, v9 row_bcast:31 row_mask:0xc bank_mask:0xf\n\t"
        "s_nop 0\n\t"
        "v_readlane_b32 s82, v9, 63\n\t"
        "s_nop 1\n\t"
        "v_fma_f32 v10, s82, -v60, v61\n\t"
        "v_rsq_f32 v10, v10\n\t"
        "s_nop 0\n\t"
        "v_pk_fma_f32 v[20:21], v[20:21], v[10:11], v[52:53] op_sel_hi:[1,0,1]\n\t"
        "v_pk_fma_f32 v[22:23], v[22:23], v[10:11], v[54:55] op_sel_hi:[1,0,1]\n\t"
        "v_pk_fma_f32 v[24:25], v[24:25], v[10:11], v[56:57] op_sel_hi:[1,0,1]\n\t"
        "v_pk_fma_f32 v[26:27], v[26:27], v[10:11], v[58:59] op_sel_hi:[1,0,1]\n\t"
        "v_cvt_pk_bf16_f32 v36, v20, v21\n\t"
        "v_cvt_pk_bf16_f32 v37, v22, v23\n\t"
        "v_cvt_pk_bf16_f32 v38, v24, v25\n\t"
        "v_cvt_pk_bf16_f32 v39, v26, v27\n\t"
        "s_bitcmp1_b32 s76, 0\n\t"
        "s_cbranch_scc1 L_sp%=\n\t"
        "s_lshr_b32 s76, s76, 1\n\t"
        "s_cmp_lt_u32 s27, 128\n\t"
        "global_store_dwordx4 v8, v[36:39], s[12:13] sc1\n\t"
        "s_cbranch_scc1 L_loop%=\n\t"
        "L_end%=:\n\t"
        "s_mov_b32 s76, 0\n\t"
        "s_cmp_lg_u32 s75, 0\n\t"
        "s_cbranch_scc1 L_pop%=\n\t"
        "s_cmp_eq_u32 s85, 0\n\t"
        "s_cbranch_scc1 L_fin%=\n\t"
        "L_wc%=:\n\t"
        "ds_read_b32 v9, v4 offset:12\n\t"
        "s_waitcnt lgkmcnt(0)\n\t"
        "v_readfirstlane_b32 s31, v9\n\t"
        "s_cmp_ge_i32 s31, 0\n\t"
        "s_cbranch_scc1 L_gc%=\n\t"
        "s_sleep 2\n\t"
        "s_branch L_wc%=\n\t"
        "L_gc%=:\n\t"
        "s_cmp_eq_u32 s31, 1\n\t"
        "s_cbranch_scc1 L_exit%=\n\t"
        "s_mov_b32 s85, 0\n\t"
        "s_mov_b32 s84, 0\n\t"
        "s_mov_b32 s24, 2\n\t"
        "v_mov_b32 v4, 0x804\n\t"
        "s_xor_b32 s26, s26, 0x2000\n\t"
        "s_branch L_body%=\n\t"
        "L_fin%=:\n\t"
        "s_cmp_lg_u32 s24, 1\n\t"
        "s_cbranch_scc1 L_exit%=\n\t"
        "s_add_u32 s86, s86, 2\n\t"
        "v_mov_b32 v9, 0\n\t"
        "v_mov_b32 v10, s86\n\t"
        "s_mov_b64 exec, 1\n\t"
        "global_store_dword v9, v10, s[88:89] sc1\n\t"
        "s_mov_b64 exec, -1\n\t"
        "s_branch L_exit%=\n\t"
        "L_sp%=:\n\t"
        "s_lshr_b32 s76, s76, 1\n\t"
        "global_store_dwordx4 v8, v[36:39], s[12:13] sc1\n\t"
        "s_cmp_ge_u32 s27, 128\n\t"
        "s_cbranch_scc1 L_end%=\n\t"
        "s_cmp_eq_u32 s74, 0\n\t"
        "s_cbranch_scc1 L_rel%=\n\t"
        "s_mov_b32 s74, 0\n\t"
        "s_waitcnt vmcnt(2)\n\t"
        "s_branch L_body%=\n\t"
        "L_rel%=:\n\t"
        "s_mov_b32 s74, 1\n\t"
        "s_lshl_b32 s76, s76, 1\n\t"
        "s_or_b32 s76, s76, 1\n\t"
        "L_pop%=:\n\t"
        "s_sub_u32 s75, s75, 1\n\t"
        "s_cmp_eq_u32 s75, 1\n\t"
        "s_cbranch_scc1 L_pop1%=\n\t"
        "v_lshlrev_b32 v28, 16, v70\n\t"
        "v_and_b32 v29, s81, v70\n\t"
        "v_lshlrev_b32 v20, 16, v74\n\t"
        "v_and_b32 v21, s81, v74\n\t"
        "v_lshlrev_b32 v30, 16, v71\n\t"
        "v_and_b32 v31, s81, v71\n\t"
        "v_lshlrev_b32 v22, 16, v75\n\t"
        "v_and_b32 v23, s81, v75\n\t"
        "v_lshlrev_b32 v32, 16, v72\n\t"
        "v_and_b32 v33, s81, v72\n\t"
        "v_lshlrev_b32 v24, 16, v76\n\t"
        "v_and_b32 v25, s81, v76\n\t"
        "v_lshlrev_b32 v34, 16, v73\n\t"
        "v_and_b32 v35, s81, v73\n\t"
        "v_lshlrev_b32 v26, 16, v77\n\t"
        "v_and_b32 v27, s81, v77\n\t"
        "s_mov_b32 s28, s71\n\t"
        "s_branch L_go%=\n\t"
        "L_pop1%=:\n\t"
        "v_lshlrev_b32 v28, 16, v62\n\t"
        "v_and_b32 v29, s81, v62\n\t"
        "v_lshlrev_b32 v20, 16, v66\n\t"
        "v_and_b32 v21, s81, v66\n\t"
        "v_lshlrev_b32 v30, 16, v63\n\t"
        "v_and_b32 v31, s81, v63\n\t"
        "v_lshlrev_b32 v22, 16, v67\n\t"
        "v_and_b32 v23, s81, v67\n\t"
        "v_lshlrev_b32 v32, 16, v64\n\t"
        "v_and_b32 v33, s81, v64\n\t"
        "v_lshlrev_b32 v24, 16, v68\n\t"
        "v_and_b32 v25, s81, v68\n\t"
        "v_lshlrev_b32 v34, 16, v65\n\t"
        "v_and_b32 v35, s81, v65\n\t"
        "v_lshlrev_b32 v26, 16, v69\n\t"
        "v_and_b32 v27, s81, v69\n\t"
        "s_mov_b32 s28, s70\n\t"
        "s_branch L_go%=\n\t"
        "L_join%=:\n\t"
        "s_cmp_eq_u32 s84, 3\n\t"
        "s_cbranch_scc1 L_pub%=\n\t"
        "s_cmp_eq_u32 s24, 1\n\t"
        "s_cselect_b32 s84, s93, 1\n\t"
        "s_mov_b32 s28, s29\n\t"
        "s_cmp_ge_u32 s27, 128\n\t"
        "s_cbranch_scc1 L_end%=\n\t"
        "s_waitcnt vmcnt(0)\n\t"
        "s_branch L_body%=\n\t"
        "L_pub%=:\n\t"
        "s_sub_u32 s87, s87, 1\n\t"
        "s_cselect_b32 s84, 1, 3\n\t"
        "s_add_u32 s86, s86, 2\n\t"
        "v_mov_b32 v9, 0\n\t"
        "v_mov_b32 v10, s86\n\t"
        "s_mov_b64 exec, 1\n\t"
        "global_store_dword v9, v10, s[88:89] sc1\n\t"
        "s_mov_b64 exec, -1\n\t"
        "s_branch L_go%=\n\t"
        "L_p%=:\n\t"
        "s_load_dword s36, s[14:15], 0x18000\n\t"
        "s_and_b32 s27, %1, 7\n\t"
        "s_lshl_b32 s27, s27, 10\n\t"
        "s_lshl_b32 s30, s22, 1\n\t"
        "s_cmp_lg_u32 s83, 2\n\t"
        "s_cselect_b32 s31, 1, 0\n\t"
        "s_xor_b32 s31, s31, s94\n\t"
        "s_xor_b32 s31, s31, s25\n\t"
        "s_or_b32 s31, s31, s30\n\t"
        "s_lshr_b32 s89, s87, 3\n\t"
        "s_lshl_b32 s89, s89, 6\n\t"
        "s_and_b32 s88, s87, 7\n\t"
        "s_or_b32 s89, s89, s88\n\t"
        "s_lshl_b32 s89, s89, 2\n\t"
        "s_add_u32 s89, s89, s27\n\t"
        "v_mov_b32 v9, s89\n\t"
        "v_mov_b32 v10, s31\n\t"
        "s_mov_b64 exec, 1\n\t"
        "global_store_dword v9, v10, s[14:15] sc1\n\t"
        "s_mov_b64 exec, -1\n\t"
        "s_add_u32 s90, s14, s27\n\t"
        "s_addc_u32 s91, s15, 0\n\t"
        "s_add_u32 s94, s30, 1000\n\t"
        "s_mov_b32 s87, 0\n\t"
        "s_waitcnt vmcnt(0) lgkmcnt(0)\n\t"
        "s_sleep 10\n\t"
        "L_p4%=:\n\t"
        "s_cmp_ge_u32 s87, 2\n\t"
        "s_cbranch_scc1 L_p6%=\n\t"
        "s_load_dwordx8 s[36:43], s[90:91], 0x0\n\t"
        "s_load_dwordx8 s[44:51], s[90:91], 0x100\n\t"
        "s_load_dwordx8 s[52:59], s[90:91], 0x200\n\t"
        "s_load_dwordx8 s[60:67], s[90:91], 0x300\n\t"
        "L_p7%=:\n\t"
        "s_xor_b32 s27, s27, 0x1000\n\t"
        "s_add_u32 s90, s14, s27\n\t"
        "s_addc_u32 s91, s15, 0\n\t"
        "s_and_b32 s80, s87, 1\n\t"
        "s_add_u32 s87, s87, 1\n\t"
        "s_waitcnt lgkmcnt(0)\n\t"
        "v_writelane_b32 v9, s36, 0\n\t"
        "v_writelane_b32 v9, s37, 1\n\t"
        "v_writelane_b32 v9, s38, 2\n\t"
        "v_writelane_b32 v9, s39, 3\n\t"
        "v_writelane_b32 v9, s40, 4\n\t"
        "v_writelane_b32 v9, s41, 5\n\t"
        "v_writelane_b32 v9, s42, 6\n\t"
        "v_writelane_b32 v9, s43, 7\n\t"
        "v_writelane_b32 v9, s44, 8\n\t"
        "v_writelane_b32 v9, s45, 9\n\t"
        "v_writelane_b32 v9, s46, 10\n\t"
        "v_writelane_b32 v9, s47, 11\n\t"
        "L_t2%=:\n\t"
        "s_cbranch_execz L_tb%=\n\t"
        "v_writelane_b32 v9, s48, 12\n\t"
        "v_writelane_b32 v9, s49, 13\n\t"
        "v_writelane_b32 v9, s50, 14\n\t"
        "v_writelane_b32 v9, s51, 15\n\t"
        "v_writelane_b32 v9, s52, 16\n\t"
        "v_writelane_b32 v9, s53, 17\n\t"
        "v_writelane_b32 v9, s54, 18\n\t"
        "v_writelane_b32 v9, s55, 19\n\t"
        "v_writelane_b32 v9, s56, 20\n\t"
        "v_writelane_b32 v9, s57, 21\n\t"
        "v_writelane_b32 v9, s58, 22\n\t"
        "v_writelane_b32 v9, s59, 23\n\t"
        "v_writelane_b32 v9, s60, 24\n\t"
        "v_writelane_b32 v9, s61, 25\n\t"
        "v_writelane_b32 v9, s62, 26\n\t"
        "v_writelane_b32 v9, s63, 27\n\t"
        "v_writelane_b32 v9, s64, 28\n\t"
        "v_writelane_b32 v9, s65, 29\n\t"
        "v_writelane_b32 v9, s66, 30\n\t"
        "v_writelane_b32 v9, s67, 31\n\t"
        "v_sub_u32 v10, s94, v9\n\t"
        "v_and_b32 v11, 1, v9\n\t"
        "v_cmp_gt_u32 vcc, 2000, v10\n\t"
        "v_cmp_eq_u32 s[38:39], 1, v11\n\t"
        "s_nop 3\n\t"
        "s_and_b32 s38, s38, vcc_lo\n\t"
        "s_and_b32 s40, vcc_lo, 0x49249249\n\t"
        "s_and_b32 s41, s38, 0x49249249\n\t"
        "s_and_b32 s42, vcc_lo, 0x92492492\n\t"
        "s_and_b32 s43, s38, 0x92492492\n\t"
        "s_and_b32 s44, vcc_lo, 0x24924924\n\t"
        "s_and_b32 s45, s38, 0x24924924\n\t"
        "s_bcnt1_i32_b32 s40, s40\n\t"
        "s_bcnt1_i32_b32 s41, s41\n\t"
        "s_bcnt1_i32_b32 s42, s42\n\t"
        "s_bcnt1_i32_b32 s43, s43\n\t"
        "s_bcnt1_i32_b32 s44, s44\n\t"
        "s_bcnt1_i32_b32 s45, s45\n\t"
        "s_lshl_b32 s41, s41, 1\n\t"
        "s_lshl_b32 s43, s43, 1\n\t"
        "s_lshl_b32 s45, s45, 1\n\t"
        "s_sub_i32 s40, s41, s40\n\t"
        "s_sub_i32 s42, s43, s42\n\t"
        "s_sub_i32 s44, s45, s44\n\t"
        "L_p8%=:\n\t"
        "s_min_i32 s41, s40, s42\n\t"
        "s_max_i32 s43, s40, s42\n\t"
        "s_min_i32 s43, s43, s44\n\t"
        "s_max_i32 s41, s41, s43\n\t"
        "s_lshl_b32 s43, s41, 7\n\t"
        "s_add_i32 s43, s43, s40\n\t"
        "s_add_i32 s43, s43, s42\n\t"
        "s_add_i32 s43, s43, s44\n\t"
        "s_cmp_gt_i32 s43, 0\n\t"
        "s_cselect_b32 s26, 1, 0\n\t"
        "s_abs_i32 s41, s41\n\t"
        "s_cmp_lt_u32 s41, 5\n\t"
        "s_cbranch_scc1 L_p9%=\n\t"
        "L_p5%=:\n\t"
        "s_xor_b32 s26, s26, s25\n\t"
        "s_lshl_b32 s26, s26, 13\n\t"
        "v_mov_b32 v9, s26\n\t"
        "ds_write_b32 v4, v9 offset:8\n\t"
        "s_setprio 0\n\t"
        "s_waitcnt lgkmcnt(0)\n\t"
        "s_barrier\n\t"
        "s_branch L_common%=\n\t"
        "L_p6%=:\n\t"
        "s_load_dwordx8 s[36:43], s[90:91], 0x0 glc\n\t"
        "s_load_dwordx8 s[44:51], s[90:91], 0x100 glc\n\t"
        "s_load_dwordx8 s[52:59], s[90:91], 0x200 glc\n\t"
        "s_load_dwordx8 s[60:67], s[90:91], 0x300 glc\n\t"
        "s_branch L_p7%=\n\t"
        "L_p9%=:\n\t"
        "s_cmp_eq_u32 s80, 1\n\t"
        "s_cbranch_scc0 L_p10%=\n\t"
        "s_mov_b32 s80, 2\n\t"
        "s_add_i32 s40, s40, s68\n\t"
        "s_add_i32 s42, s42, s69\n\t"
        "s_add_i32 s44, s44, s70\n\t"
        "s_branch L_p8%=\n\t"
        "L_p10%=:\n\t"
        "s_mov_b32 s68, s40\n\t"
        "s_mov_b32 s69, s42\n\t"
        "s_mov_b32 s70, s44\n\t"
        "s_cmp_lt_u32 s87, 4\n\t"
        "s_cbranch_scc1 L_p4%=\n\t"
        "s_branch L_p5%=\n\t"
        "L_m2%=:\n\t"
        "ds_read_b32 v9, v4 offset:8\n\t"
        "s_waitcnt vmcnt(0) lgkmcnt(0)\n\t"
        "v_readfirstlane_b32 s30, v9\n\t"
        "v_readfirstlane_b32 s87, v21\n\t"
        "s_lshr_b32 s30, s30, 13\n\t"
        "s_mov_b64 exec, 1\n\t"
        "s_cmp_eq_u32 s85, 0\n\t"
        "s_cbranch_scc0 L_m4%=\n\t"
        "s_add_u32 s86, s87, 2\n\t"
        "s_and_b32 s86, s86, -2\n\t"
        "s_or_b32 s86, s86, s30\n\t"
        "s_mov_b32 s93, 3\n\t"
        "s_mov_b32 s87, 1\n\t"
        "v_mov_b32 v10, s86\n\t"
        "global_store_dword v5, v10, s[88:89] sc1\n\t"
        "s_branch L_m7%=\n\t"
        "L_m4%=:\n\t"
        "s_mov_b32 s27, 0\n\t"
        "s_mov_b32 s31, 2\n\t"
        "L_m5%=:\n\t"
        "global_load_dword v21, v5, s[88:89] sc1\n\t"
        "s_waitcnt vmcnt(0)\n\t"
        "v_readfirstlane_b32 s86, v21\n\t"
        "s_cmp_lg_u32 s86, s87\n\t"
        "s_cbranch_scc1 L_m8%=\n\t"
        "s_add_u32 s27, s27, 1\n\t"
        "s_cmp_ge_u32 s27, 96\n\t"
        "s_cbranch_scc1 L_m6%=\n\t"
        "s_sleep 4\n\t"
        "s_branch L_m5%=\n\t"
        "L_m8%=:\n\t"
        "s_and_b32 s86, s86, 1\n\t"
        "s_cmp_lg_u32 s86, s30\n\t"
        "s_cselect_b32 s31, 1, 2\n\t"
        "s_cbranch_scc1 L_m6%=\n\t"
        "v_mov_b32 v11, 128\n\t"
        "ds_max_u32 v4, v11\n\t"
        "L_m6%=:\n\t"
        "v_mov_b32 v10, s31\n\t"
        "ds_write_b32 v4, v10 offset:12\n\t"
        "L_m7%=:\n\t"
        "s_mov_b64 exec, -1\n\t"
        "s_setprio 0\n\t"
        "s_branch L_common%=\n\t"
        "L_exit%=:\n\t"
        :
        : "s"(__builtin_amdgcn_kernarg_segment_ptr()), "s"(blockIdx.x), "v"(threadIdx.x)
        : "memory", "scc", "vcc",
          "s8", "s9", "s10", "s11", "s12", "s13", "s14", "s15", "s16", "s17", "s18", "s19", "s20", "s21", "s22", "s23",
          "s24", "s25", "s26", "s27", "s28", "s29", "s30", "s31", "s34", "s35", "s36", "s37", "s38", "s39", "s40", "s41",
          "s42", "s43", "s44", "s45", "s46", "s47", "s48", "s49", "s50", "s51", "s52", "s53", "s54", "s55", "s56", "s57",
          "s58", "s59", "s60", "s61", "s62", "s63", "s64", "s65", "s66", "s67", "s68", "s69", "s70", "s71", "s74", "s75",
          "s76", "s80", "s81", "s82", "s83", "s84", "s85", "s86", "s87", "s88", "s89", "s90", "s91", "s93", "s94",
          "v2", "v3", "v4", "v5", "v6", "v7", "v8", "v9", "v10", "v11", "v12", "v13", "v14", "v15", "v16", "v17",
          "v18", "v19", "v20", "v21", "v22", "v23", "v24", "v25", "v26", "v27", "v28", "v29", "v30", "v31", "v32", "v33",
          "v34", "v35", "v36", "v37", "v38", "v39", "v40", "v41", "v42", "v43", "v44", "v45", "v46", "v47", "v48", "v49",
          "v50", "v51", "v52", "v53", "v54", "v55", "v56", "v57", "v58", "v59", "v60", "v61", "v62", "v63", "v64", "v65",
          "v66", "v67", "v68", "v69", "v70", "v71", "v72", "v73", "v74", "v75", "v76", "v77");
    asm volatile("" ::"v"((__attribute__((address_space(3))) uint4*)lds));
}

__global__ __launch_bounds__(256) void deepnorm_generic(const __hip_bfloat16* x, const __hip_bfloat16* f,
                                                        __hip_bfloat16* out, const __hip_bfloat16* g,
                                                        const __hip_bfloat16* b, const __hip_bfloat16* alpha,
                                                        int rows, int d) {
    const int lane = threadIdx.x & 63;
    const int row = blockIdx.x * 4 + (threadIdx.x >> 6);
    if (row >= rows) return;
    const float a = __bfloat162float(alpha[0]);
    const size_t base = (size_t)row * d;
    float s = 0.f;
    for (int j = lane; j < d; j += 64) s += a * __bfloat162float(x[base + j]) + __bfloat162float(f[base + j]);
    for (int o = 32; o > 0; o >>= 1) s += __shfl_xor(s, o, 64);
    const float mean = s / d;
    float q = 0.f;
    for (int j = lane; j < d; j += 64) {
        const float h = a * __bfloat162float(x[base + j]) + __bfloat162float(f[base + j]) - mean;
        q += h * h;
    }
    for (int o = 32; o > 0; o >>= 1) q += __shfl_xor(q, o, 64);
    const float rstd = rsqrtf(q / d + 1e-5f);
    for (int j = lane; j < d; j += 64) {
        const float h = a * __bfloat162float(x[base + j]) + __bfloat162float(f[base + j]) - mean;
        out[base + j] = __float2bfloat16(h * rstd * __bfloat162float(g[j]) + __bfloat162float(b[j]));
    }
}

extern "C" void kernel_launch(void* const* d_in, const int* in_sizes, int n_in, void* d_out, int out_size,
                              void* d_ws, size_t ws_size, hipStream_t stream) {
    const int d = in_sizes[2];
    const int rows = in_sizes[0] / d;
    if (d == 512 && rows == 32768 && ws_size >= (1u << 20)) {
        deepnorm_kernel<<<256, 1024, 0, stream>>>(d_in[0], d_in[1], d_out, d_ws, d_in[2], d_in[3], d_in[4]);
    } else {
        deepnorm_generic<<<(rows + 3) / 4, 256, 0, stream>>>(
            (const __hip_bfloat16*)d_in[0], (const __hip_bfloat16*)d_in[1], (__hip_bfloat16*)d_out,
            (const __hip_bfloat16*)d_in[2], (const __hip_bfloat16*)d_in[3], (const __hip_bfloat16*)d_in[4], rows, d);
    }
}
```
